```python
import torch

def _softmax(x, axis=-1):
    return torch.softmax(x, dim=axis)
def _gelu(x):
    return torch.nn.functional.gelu(x, approximate="tanh")
def _silu(x):
    return torch.nn.functional.silu(x)
def _sigmoid(x):
    return torch.sigmoid(x)
def _layernorm(x, axis=-1, eps=1e-5):
    m = x.mean(dim=axis, keepdim=True)
    v = x.var(dim=axis, keepdim=True, unbiased=False)
    return (x - m) / torch.sqrt(v + eps)
def _scatter_add(tgt, idx, src, axis=0):
    return tgt.clone().index_add_(axis, idx, src)
def _segment_sum(src, seg, n):
    out = torch.zeros((n,) + src.shape[1:], dtype=src.dtype)
    return out.index_add_(0, seg, src)
def _pad(x, pw):
    flat = tuple(v for pair in reversed(pw) for v in pair)
    return torch.nn.functional.pad(x, flat)

kernel_name = 'interleave_layers__linear_attn_block__mixer_channel_mlp__prompt'

def setup_inputs():

    _w = (lambda a: (lambda t: t.to(torch.bfloat16) if t.is_floating_point() else t)(torch.as_tensor(a)))
    def _A_setup():
        B, S, D = 1, 2048, 1024
        return {
            "x": _w((lambda *s: torch.randn(*s, dtype=torch.bfloat16) * 0.05)(B, S, D)),
            "wqkv": _w((lambda *s: torch.randn(*s, dtype=torch.bfloat16) * 0.05)(D, 3 * D)),
            "wo": _w((lambda *s: torch.randn(*s, dtype=torch.bfloat16) * 0.05)(D, D)),
        }
    _A_in = _A_setup()

    def _B_setup():
        B, S, D, F = 1, 2048, 1024, 3584
        return {
            "x": _w((lambda *s: torch.randn(*s, dtype=torch.bfloat16) * 0.05)(B, S, D)),
            "w1": _w((lambda *s: torch.randn(*s, dtype=torch.bfloat16) * 3.2)(D, F) * (D ** -0.5)),
            "b1": _w((lambda *s: torch.randn(*s, dtype=torch.bfloat16) * 0.4)(F)),
            "w2": _w((lambda *s: torch.randn(*s, dtype=torch.bfloat16) * 0.4)(F, D) * (F ** -0.5)),
            "b2": _w((lambda *s: torch.randn(*s, dtype=torch.bfloat16) * 0.4)(D)),
        }
    _B_in = _B_setup()

    return {
        "A_x": _A_in["x"],
        "A_wqkv": _A_in["wqkv"],
        "A_wo": _A_in["wo"],
        "B_x": _B_in["x"],
        "B_w1": _B_in["w1"],
        "B_b1": _B_in["b1"],
        "B_w2": _B_in["w2"],
        "B_b2": _B_in["b2"],
    }

def reference(A_x, A_wqkv, A_wo, B_x, B_w1, B_b1, B_w2, B_b2):
    def _A_ref(x, wqkv, wo):
        B, S, D = x.shape
        H = 32; Dh = D // H
        qkv = (lambda x, *d: x.permute(*d))((x @ wqkv).reshape(B, S, 3, H, Dh), 2, 0, 3, 1, 4)
        q, k, v = qkv[0], qkv[1], qkv[2]
        phi = lambda t: torch.where(t > 0, t + 1, torch.exp(t)) + 1e-6
        pq, pk = phi(q), phi(k)
        kv = torch.einsum("bhsd,bhse->bhde", pk, v)
        z = torch.einsum("bhsd->bhd", pk)
        num = torch.einsum("bhqd,bhde->bhqe", pq, kv)
        den = torch.einsum("bhqd,bhd->bhq", pq, z)[..., None] + 1e-6
        o = num / den
        return (lambda x, *d: x.permute(*d))(o, 0, 2, 1, 3).reshape(B, S, D) @ wo

    def _B_ref(x, w1, b1, w2, b2):
        y = _layernorm(x, axis=-1)
        h = _gelu(torch.einsum("bsd,df->bsf", y, w1) + b1)
        return torch.einsum("bsf,fd->bsd", h, w2) + b2

    x = A_x
    x = x + _A_ref(_layernorm(x, axis=-1), A_wqkv, A_wo)
    x = x + _B_ref(_layernorm(x, axis=-1), B_w1, B_b1, B_w2, B_b2)
    x = x + _A_ref(_layernorm(x, axis=-1), A_wqkv, A_wo)
    x = x + _B_ref(_layernorm(x, axis=-1), B_w1, B_b1, B_w2, B_b2)
    return x
```

```cpp
#include <hip/hip_runtime.h>
#include <hip/hip_bf16.h>
#include <stdint.h>

typedef unsigned short u16;
using bf16x8 = __attribute__((ext_vector_type(8))) short;
using s16x4  = __attribute__((ext_vector_type(4))) short;
using f32x4  = __attribute__((ext_vector_type(4))) float;
using u16x8  = __attribute__((ext_vector_type(8))) unsigned short;
typedef short v4i16_t __attribute__((ext_vector_type(4)));
#define LAS __attribute__((address_space(3)))

static constexpr int S_ = 2048, D_ = 1024, H_ = 32, DH_ = 32, F_ = 3584;

__device__ __forceinline__ float bf2f(u16 u) { return __uint_as_float(((unsigned)u) << 16); }
__device__ __forceinline__ u16 f2bf(float f) {
  unsigned u = __float_as_uint(f);
  u += 0x7fffu + ((u >> 16) & 1u);
  return (u16)(u >> 16);
}
__device__ __forceinline__ float rbf(float f) { return bf2f(f2bf(f)); }

template <int TWICE>
__global__ __launch_bounds__(256) void ln_kernel(const u16* __restrict__ x, u16* __restrict__ y) {
  const int row = blockIdx.x * 4 + (threadIdx.x >> 6);
  const int lane = threadIdx.x & 63;
  const u16x8* xp = reinterpret_cast<const u16x8*>(x + (size_t)row * D_);
  u16x8 a = xp[lane], b = xp[lane + 64];
  float v[16];
#pragma unroll
  for (int i = 0; i < 8; ++i) { v[i] = bf2f(a[i]); v[8 + i] = bf2f(b[i]); }
#pragma unroll
  for (int pass = 0; pass < (TWICE ? 2 : 1); ++pass) {
    float s = 0.f;
#pragma unroll
    for (int i = 0; i < 16; ++i) s += v[i];
#pragma unroll
    for (int o = 32; o >= 1; o >>= 1) s += __shfl_xor(s, o);
    const float m = s * (1.f / D_);
    float q = 0.f;
#pragma unroll
    for (int i = 0; i < 16; ++i) { float d = v[i] - m; q += d * d; }
#pragma unroll
    for (int o = 32; o >= 1; o >>= 1) q += __shfl_xor(q, o);
    const float r = rsqrtf(q * (1.f / D_) + 1e-5f);
#pragma unroll
    for (int i = 0; i < 16; ++i) v[i] = rbf((v[i] - m) * r);
  }
  u16x8 oa, ob;
#pragma unroll
  for (int i = 0; i < 8; ++i) { oa[i] = f2bf(v[i]); ob[i] = f2bf(v[8 + i]); }
  u16x8* yp = reinterpret_cast<u16x8*>(y + (size_t)row * D_);
  yp[lane] = oa; yp[lane + 64] = ob;
}

enum { EPI_QKV = 0, EPI_GELU = 1, EPI_RES = 2, EPI_RES_BIAS = 3 };

template <int BN, int WN>
struct GemmCfg {
  static constexpr int BM = 128, BK = 64, NST = 3;
  static constexpr int THREADS = 128 * WN;
  static constexpr int NWAVES = 2 * WN;
  static constexpr int A_BYTES = BM * BK * 2;
  static constexpr int B_BYTES = BN * BK * 2;
  static constexpr int STAGE = A_BYTES + B_BYTES;
  static constexpr int A_PIECES = A_BYTES / 1024, B_PIECES = B_BYTES / 1024;
  static constexpr int A_PW = A_PIECES / NWAVES, B_PW = B_PIECES / NWAVES;
  static constexpr int LOADS = A_PW + B_PW;
  static constexpr int WTN = BN / WN;
  static constexpr int MF = 4, NF = WTN / 16;
  static constexpr int CPAD = 8;
  static constexpr int C_BYTES = BM * (BN + CPAD) * 2;
  static constexpr int LDS_BYTES = (NST * STAGE > C_BYTES) ? NST * STAGE : C_BYTES;
};

__device__ __forceinline__ int bswz(int k) { return ((k >> 1) & 1) | (((k >> 3) & 1) << 1); }

__device__ __forceinline__ float gelu_tanh(float x) {
  const float u = 0.7978845608028654f * (x + 0.044715f * x * x * x);
  const float e = __expf(2.f * u);
  const float t = 1.f - 2.f / (e + 1.f);
  return 0.5f * x * (1.f + t);
}

template <int BN, int WN, int EPI>
__global__ __launch_bounds__(128 * WN) void gemm_kernel(const u16* __restrict__ A, const u16* __restrict__ B,
                                                         u16* __restrict__ C, const u16* __restrict__ bias,
                                                         const u16* __restrict__ res, int N, int K, int ntn) {
  using Cfg = GemmCfg<BN, WN>;
  extern __shared__ __attribute__((aligned(16))) char smem[];
  const int tid = threadIdx.x, lane = tid & 63, wid = tid >> 6;
  const int wr = wid / WN, wc = wid % WN;
  const int bm = blockIdx.x / ntn, bn = blockIdx.x % ntn;
  const int m0 = bm * Cfg::BM, n0 = bn * BN;

  const char* asrc[Cfg::A_PW];
  const char* bsrc[Cfg::B_PW];
#pragma unroll
  for (int j = 0; j < Cfg::A_PW; ++j) {
    const int p = wid * Cfg::A_PW + j;
    const int row = p * 8 + (lane >> 3), slot = lane & 7;
    const int chunk = slot ^ (row & 7);
    asrc[j] = reinterpret_cast<const char*>(A + (size_t)(m0 + row) * K + chunk * 8);
  }
#pragma unroll
  for (int j = 0; j < Cfg::B_PW; ++j) {
    const int q = wid * Cfg::B_PW + j;
    const int panel = q >> 3, kr = (q & 7) * 8 + (lane >> 3), s = lane & 7;
    const int c32 = (s >> 1) ^ bswz(kr);
    bsrc[j] = reinterpret_cast<const char*>(B + (size_t)kr * N + n0 + panel * 64 + c32 * 16 + (s & 1) * 8);
  }
  const size_t a_step = (size_t)Cfg::BK * 2;
  const size_t b_step = (size_t)Cfg::BK * N * 2;

  auto stage_load = [&](int buf) {
    char* base = smem + buf * Cfg::STAGE;
#pragma unroll
    for (int j = 0; j < Cfg::A_PW; ++j) {
      __builtin_amdgcn_global_load_lds((const LAS void*)asrc[j], (LAS void*)(base + (wid * Cfg::A_PW + j) * 1024), 16, 0, 0);
      asrc[j] += a_step;
    }
#pragma unroll
    for (int j = 0; j < Cfg::B_PW; ++j) {
      __builtin_amdgcn_global_load_lds((const LAS void*)bsrc[j], (LAS void*)(base + Cfg::A_BYTES + (wid * Cfg::B_PW + j) * 1024), 16, 0, 0);
      bsrc[j] += b_step;
    }
  };

  const int g = lane >> 4, fr = lane & 15;
  int a_off[2];
#pragma unroll
  for (int kk = 0; kk < 2; ++kk) a_off[kk] = (wr * 64 + fr) * 128 + (((kk * 4 + g) ^ (fr & 7)) * 16);
  const int tq = fr >> 2, tp = fr & 3;
  const int bf = (tq >> 1) | ((g & 1) << 1);
  int b_off[Cfg::NF];
#pragma unroll
  for (int ni = 0; ni < Cfg::NF; ++ni) {
    const int col0 = wc * Cfg::WTN + ni * 16;
    const int panel = col0 >> 6, c32 = (col0 & 63) >> 4;
    b_off[ni] = Cfg::A_BYTES + panel * 8192 + (8 * g + tq) * 128 + ((c32 ^ bf) * 32) + tp * 8;
  }

  f32x4 acc[Cfg::MF][Cfg::NF];
#pragma unroll
  for (int mi = 0; mi < Cfg::MF; ++mi)
#pragma unroll
    for (int ni = 0; ni < Cfg::NF; ++ni) acc[mi][ni] = f32x4{0.f, 0.f, 0.f, 0.f};

  const int nk = K / Cfg::BK;
  stage_load(0);
  stage_load(1);
  int buf = 0;
  for (int kt = 0; kt < nk; ++kt) {
    if (kt + 1 < nk) asm volatile("s_waitcnt vmcnt(%0)" ::"i"(Cfg::LOADS) : "memory");
    else asm volatile("s_waitcnt vmcnt(0)" ::: "memory");
    __builtin_amdgcn_s_barrier();
    if (kt + 2 < nk) stage_load(buf >= 1 ? buf - 1 : 2);
    const char* sb = smem + buf * Cfg::STAGE;
#pragma unroll
    for (int kk = 0; kk < 2; ++kk) {
      bf16x8 af[Cfg::MF], bfr[Cfg::NF];
#pragma unroll
      for (int ni = 0; ni < Cfg::NF; ++ni) {
        const char* p = sb + b_off[ni] + kk * 32 * 128;
        v4i16_t lo = __builtin_amdgcn_ds_read_tr16_b64_v4i16((LAS v4i16_t*)(p));
        v4i16_t hi = __builtin_amdgcn_ds_read_tr16_b64_v4i16((LAS v4i16_t*)(p + 4 * 128));
        bfr[ni] = bf16x8{lo[0], lo[1], lo[2], lo[3], hi[0], hi[1], hi[2], hi[3]};
      }
#pragma unroll
      for (int mi = 0; mi < Cfg::MF; ++mi)
        af[mi] = *reinterpret_cast<const bf16x8*>(sb + a_off[kk] + mi * 16 * 128);
#pragma unroll
      for (int mi = 0; mi < Cfg::MF; ++mi)
#pragma unroll
        for (int ni = 0; ni < Cfg::NF; ++ni)
          acc[mi][ni] = __builtin_amdgcn_mfma_f32_16x16x32_bf16(af[mi], bfr[ni], acc[mi][ni], 0, 0, 0);
    }
    buf = (buf == 2) ? 0 : buf + 1;
  }

  __builtin_amdgcn_s_barrier();
  u16* cs = reinterpret_cast<u16*>(smem);
  constexpr int CLD = BN + Cfg::CPAD;
#pragma unroll
  for (int ni = 0; ni < Cfg::NF; ++ni) {
    const int col = wc * Cfg::WTN + ni * 16 + fr;
    float bv = 0.f;
    if (EPI == EPI_GELU) bv = bf2f(bias[n0 + col]);
#pragma unroll
    for (int mi = 0; mi < Cfg::MF; ++mi) {
#pragma unroll
      for (int r = 0; r < 4; ++r) {
        const int row = wr * 64 + mi * 16 + g * 4 + r;
        float v = rbf(acc[mi][ni][r]);
        if (EPI == EPI_QKV) {
          if (n0 + col < 2 * D_) {
            v = (v > 0.f) ? rbf(v + 1.f) : rbf(__expf(v));
            v = v + 1e-6f;
          }
        } else if (EPI == EPI_GELU) {
          v = gelu_tanh(rbf(v + bv));
        }
        cs[row * CLD + col] = f2bf(v);
      }
    }
  }
  __builtin_amdgcn_s_waitcnt(0xc07f);
  __builtin_amdgcn_s_barrier();
  constexpr int CPR = BN / 8;
  constexpr int NCH = Cfg::BM * CPR;
#pragma unroll
  for (int i = tid; i < NCH; i += Cfg::THREADS) {
    const int row = i / CPR, c8 = (i % CPR) * 8;
    u16x8 v = *reinterpret_cast<const u16x8*>(cs + row * CLD + c8);
    const size_t go = (size_t)(m0 + row) * N + n0 + c8;
    if (EPI == EPI_RES || EPI == EPI_RES_BIAS) {
      u16x8 rv = *reinterpret_cast<const u16x8*>(res + go);
      u16x8 bb;
      if (EPI == EPI_RES_BIAS) bb = *reinterpret_cast<const u16x8*>(bias + n0 + c8);
#pragma unroll
      for (int e = 0; e < 8; ++e) {
        float t = bf2f(v[e]);
        if (EPI == EPI_RES_BIAS) t = rbf(t + bf2f(bb[e]));
        v[e] = f2bf(bf2f(rv[e]) + t);
      }
    }
    *reinterpret_cast<u16x8*>(C + go) = v;
  }
}

__global__ __launch_bounds__(256) void kv_kernel(const u16* __restrict__ qkv, float* __restrict__ part) {
  __shared__ float sk[64][33], sv[64][32];
  const int h = blockIdx.x, ch = blockIdx.y, t = threadIdx.x;
  const int d = t >> 3, e4 = (t & 7) * 4;
  float acc[4] = {0.f, 0.f, 0.f, 0.f}, z = 0.f;
  for (int r0 = 0; r0 < 256; r0 += 64) {
    __syncthreads();
    {
      const int row = t >> 2, c8 = (t & 3) * 8;
      const size_t base = (size_t)(ch * 256 + r0 + row) * (3 * D_) + h * DH_ + c8;
      u16x8 kk = *reinterpret_cast<const u16x8*>(qkv + base + D_);
      u16x8 vv = *reinterpret_cast<const u16x8*>(qkv + base + 2 * D_);
#pragma unroll
      for (int i = 0; i < 8; ++i) { sk[row][c8 + i] = bf2f(kk[i]); sv[row][c8 + i] = bf2f(vv[i]); }
    }
    __syncthreads();
#pragma unroll 8
    for (int r = 0; r < 64; ++r) {
      const float kd = sk[r][d];
      const f32x4 v4 = *reinterpret_cast<const f32x4*>(&sv[r][e4]);
      acc[0] += kd * v4[0]; acc[1] += kd * v4[1]; acc[2] += kd * v4[2]; acc[3] += kd * v4[3];
      z += kd;
    }
  }
  float* p = part + ((size_t)ch * H_ + h) * 1056;
  *reinterpret_cast<f32x4*>(p + d * 32 + e4) = f32x4{acc[0], acc[1], acc[2], acc[3]};
  if ((t & 7) == 0) p[1024 + d] = z;
}

__global__ __launch_bounds__(256) void o_kernel(const u16* __restrict__ qkv, const float* __restrict__ part,
                                                u16* __restrict__ o) {
  __shared__ float skv[32][32], sz[32];
  const int h = blockIdx.x, rb = blockIdx.y, t = threadIdx.x;
  for (int i = t; i < 1056; i += 256) {
    float s = 0.f;
#pragma unroll
    for (int c = 0; c < 8; ++c) s += part[((size_t)c * H_ + h) * 1056 + i];
    s = rbf(s);
    if (i < 1024) skv[i >> 5][i & 31] = s; else sz[i - 1024] = s;
  }
  __syncthreads();
  const int row = rb * 256 + t;
  const u16x8* qp = reinterpret_cast<const u16x8*>(qkv + (size_t)row * (3 * D_) + h * DH_);
  float q[32];
#pragma unroll
  for (int c = 0; c < 4; ++c) {
    u16x8 a = qp[c];
#pragma unroll
    for (int i = 0; i < 8; ++i) q[c * 8 + i] = bf2f(a[i]);
  }
  float den = 0.f;
#pragma unroll
  for (int d = 0; d < 32; ++d) den += q[d] * sz[d];
  den = rbf(rbf(den) + 1e-6f);
  u16x8 ov[4];
#pragma unroll
  for (int e = 0; e < 32; ++e) {
    float num = 0.f;
#pragma unroll
    for (int d = 0; d < 32; ++d) num += q[d] * skv[d][e];
    ov[e >> 3][e & 7] = f2bf(rbf(num) / den);
  }
  u16x8* op = reinterpret_cast<u16x8*>(o + (size_t)row * D_ + h * DH_);
#pragma unroll
  for (int c = 0; c < 4; ++c) op[c] = ov[c];
}

template <int BN, int WN, int EPI>
static void launch_gemm(const u16* A, const u16* B, u16* C, const u16* bias, const u16* res, int N, int K,
                        hipStream_t stream) {
  using Cfg = GemmCfg<BN, WN>;
  static bool attr = false;
  if (!attr) {
    hipFuncSetAttribute((const void*)gemm_kernel<BN, WN, EPI>, hipFuncAttributeMaxDynamicSharedMemorySize, Cfg::LDS_BYTES);
    attr = true;
  }
  const int ntn = N / BN;
  gemm_kernel<BN, WN, EPI><<<(S_ / 128) * ntn, Cfg::THREADS, Cfg::LDS_BYTES, stream>>>(A, B, C, bias, res, N, K, ntn);
}

extern "C" void kernel_launch(void* const* d_in, const int* in_sizes, int n_in, void* d_out, int out_size,
                              void* d_ws, size_t ws_size, hipStream_t stream) {
  const u16* x0 = (const u16*)d_in[0];
  const u16* wqkv = (const u16*)d_in[1];
  const u16* wo = (const u16*)d_in[2];
  const u16* w1 = (const u16*)d_in[4];
  const u16* b1 = (const u16*)d_in[5];
  const u16* w2 = (const u16*)d_in[6];
  const u16* b2 = (const u16*)d_in[7];
  u16* out = (u16*)d_out;
  char* ws = (char*)d_ws;
  u16* xn = (u16*)(ws);
  u16* qkv = (u16*)(ws + (4u << 20));
  u16* ob = (u16*)(ws + (16u << 20));
  u16* xs = (u16*)(ws + (20u << 20));
  u16* hb = (u16*)(ws + (24u << 20));
  float* part = (float*)(ws + (40u << 20));

  for (int layer = 0; layer < 2; ++layer) {
    const u16* xin = layer == 0 ? x0 : xs;
    ln_kernel<0><<<S_ / 4, 256, 0, stream>>>(xin, xn);
    launch_gemm<192, 4, EPI_QKV>(xn, wqkv, qkv, nullptr, nullptr, 3 * D_, D_, stream);
    kv_kernel<<<dim3(H_, 8), 256, 0, stream>>>(qkv, part);
    o_kernel<<<dim3(H_, 8), 256, 0, stream>>>(qkv, part, ob);
    launch_gemm<64, 2, EPI_RES>(ob, wo, xs, nullptr, xin, D_, D_, stream);
    ln_kernel<1><<<S_ / 4, 256, 0, stream>>>(xs, xn);
    launch_gemm<256, 4, EPI_GELU>(xn, w1, hb, b1, nullptr, F_, D_, stream);
    launch_gemm<64, 2, EPI_RES_BIAS>(hb, w2, layer == 1 ? out : xs, b2, xs, D_, F_, stream);
  }
}
```

```cpp
#include <hip/hip_runtime.h>
#include <hip/hip_bf16.h>
#include <stdint.h>

typedef unsigned short u16;
using bf16x8 = __attribute__((ext_vector_type(8))) short;
using s16x4  = __attribute__((ext_vector_type(4))) short;
using f32x4  = __attribute__((ext_vector_type(4))) float;
using u16x8  = __attribute__((ext_vector_type(8))) unsigned short;
typedef short v4i16_t __attribute__((ext_vector_type(4)));
#define LAS __attribute__((address_space(3)))

static constexpr int S_ = 2048, D_ = 1024, H_ = 32, DH_ = 32, F_ = 3584;

__device__ __forceinline__ float bf2f(u16 u) { return __uint_as_float(((unsigned)u) << 16); }
__device__ __forceinline__ u16 f2bf(float f) {
  unsigned u = __float_as_uint(f);
  u += 0x7fffu + ((u >> 16) & 1u);
  return (u16)(u >> 16);
}
__device__ __forceinline__ float rbf(float f) { return bf2f(f2bf(f)); }

template <int TWICE>
__global__ __launch_bounds__(256) void ln_kernel(const u16* __restrict__ x, u16* __restrict__ y) {
  const int row = blockIdx.x * 4 + (threadIdx.x >> 6);
  const int lane = threadIdx.x & 63;
  const u16x8* xp = reinterpret_cast<const u16x8*>(x + (size_t)row * D_);
  u16x8 a = xp[lane], b = xp[lane + 64];
  float v[16];
#pragma unroll
  for (int i = 0; i < 8; ++i) { v[i] = bf2f(a[i]); v[8 + i] = bf2f(b[i]); }
#pragma unroll
  for (int pass = 0; pass < (TWICE ? 2 : 1); ++pass) {
    float s = 0.f;
#pragma unroll
    for (int i = 0; i < 16; ++i) s += v[i];
#pragma unroll
    for (int o = 32; o >= 1; o >>= 1) s += __shfl_xor(s, o);
    const float m = s * (1.f / D_);
    float q = 0.f;
#pragma unroll
    for (int i = 0; i < 16; ++i) { float d = v[i] - m; q += d * d; }
#pragma unroll
    for (int o = 32; o >= 1; o >>= 1) q += __shfl_xor(q, o);
    const float r = rsqrtf(q * (1.f / D_) + 1e-5f);
#pragma unroll
    for (int i = 0; i < 16; ++i) v[i] = rbf((v[i] - m) * r);
  }
  u16x8 oa, ob;
#pragma unroll
  for (int i = 0; i < 8; ++i) { oa[i] = f2bf(v[i]); ob[i] = f2bf(v[8 + i]); }
  u16x8* yp = reinterpret_cast<u16x8*>(y + (size_t)row * D_);
  yp[lane] = oa; yp[lane + 64] = ob;
}

enum { EPI_QKV = 0, EPI_GELU = 1, EPI_RES = 2, EPI_RES_BIAS = 3 };

template <int BN, int WN>
struct GemmCfg {
  static constexpr int BM = 128, BK = 64, NST = 3;
  static constexpr int THREADS = 128 * WN;
  static constexpr int NWAVES = 2 * WN;
  static constexpr int A_BYTES = BM * BK * 2;
  static constexpr int B_BYTES = BN * BK * 2;
  static constexpr int STAGE = A_BYTES + B_BYTES;
  static constexpr int A_PIECES = A_BYTES / 1024, B_PIECES = B_BYTES / 1024;
  static constexpr int A_PW = A_PIECES / NWAVES, B_PW = B_PIECES / NWAVES;
  static constexpr int LOADS = A_PW + B_PW;
  static constexpr int WTN = BN / WN;
  static constexpr int MF = 4, NF = WTN / 16;
  static constexpr int CPAD = 8;
  static constexpr int C_BYTES = BM * (BN + CPAD) * 2;
  static constexpr int LDS_BYTES = (NST * STAGE > C_BYTES) ? NST * STAGE : C_BYTES;
};

__device__ __forceinline__ void glds16(const void* g, unsigned lds_base) {
  unsigned sv;
  asm volatile("s_mov_b32 %0, m0\n\ts_mov_b32 m0, %2\n\ts_nop 0\n\tglobal_load_lds_dwordx4 %1, off\n\ts_mov_b32 m0, %0"
               : "=&s"(sv) : "v"(g), "s"(lds_base) : "memory");
}

__device__ __forceinline__ int bswz(int k) { return ((k >> 1) & 1) | (((k >> 3) & 1) << 1); }

__device__ __forceinline__ float gelu_tanh(float x) {
  const float u = 0.7978845608028654f * (x + 0.044715f * x * x * x);
  const float e = __expf(2.f * u);
  const float t = 1.f - 2.f / (e + 1.f);
  return 0.5f * x * (1.f + t);
}

template <int BN, int WN, int EPI>
__global__ __launch_bounds__(128 * WN) void gemm_kernel(const u16* __restrict__ A, const u16* __restrict__ B,
                                                         u16* __restrict__ C, const u16* __restrict__ bias,
                                                         const u16* __restrict__ res, int N, int K, int ntn) {
  using Cfg = GemmCfg<BN, WN>;
  extern __shared__ __attribute__((aligned(16))) char smem[];
  const int tid = threadIdx.x, lane = tid & 63, wid = __builtin_amdgcn_readfirstlane(tid >> 6);
  const int wr = wid / WN, wc = wid % WN;
  const unsigned smem_base = (unsigned)(size_t)(LAS char*)smem;
  const int bm = blockIdx.x / ntn, bn = blockIdx.x % ntn;
  const int m0 = bm * Cfg::BM, n0 = bn * BN;

  const char* asrc[Cfg::A_PW];
  const char* bsrc[Cfg::B_PW];
#pragma unroll
  for (int j = 0; j < Cfg::A_PW; ++j) {
    const int p = wid * Cfg::A_PW + j;
    const int row = p * 8 + (lane >> 3), slot = lane & 7;
    const int chunk = slot ^ (row & 7);
    asrc[j] = reinterpret_cast<const char*>(A + (size_t)(m0 + row) * K + chunk * 8);
  }
#pragma unroll
  for (int j = 0; j < Cfg::B_PW; ++j) {
    const int q = wid * Cfg::B_PW + j;
    const int panel = q >> 3, kr = (q & 7) * 8 + (lane >> 3), s = lane & 7;
    const int c32 = (s >> 1) ^ bswz(kr);
    bsrc[j] = reinterpret_cast<const char*>(B + (size_t)kr * N + n0 + panel * 64 + c32 * 16 + (s & 1) * 8);
  }
  const size_t a_step = (size_t)Cfg::BK * 2;
  const size_t b_step = (size_t)Cfg::BK * N * 2;

  auto stage_load = [&](int buf) {
    const unsigned base = smem_base + buf * Cfg::STAGE;
#pragma unroll
    for (int j = 0; j < Cfg::A_PW; ++j) {
      glds16(asrc[j], base + (wid * Cfg::A_PW + j) * 1024);
      asrc[j] += a_step;
    }
#pragma unroll
    for (int j = 0; j < Cfg::B_PW; ++j) {
      glds16(bsrc[j], base + Cfg::A_BYTES + (wid * Cfg::B_PW + j) * 1024);
      bsrc[j] += b_step;
    }
  };

  const int g = lane >> 4, fr = lane & 15;
  int a_off[2];
#pragma unroll
  for (int kk = 0; kk < 2; ++kk) a_off[kk] = (wr * 64 + fr) * 128 + (((kk * 4 + g) ^ (fr & 7)) * 16);
  const int tq = fr >> 2, tp = fr & 3;
  const int bf = (tq >> 1) | ((g & 1) << 1);
  int b_off[Cfg::NF];
#pragma unroll
  for (int ni = 0; ni < Cfg::NF; ++ni) {
    const int col0 = wc * Cfg::WTN + ni * 16;
    const int panel = col0 >> 6, c32 = (col0 & 63) >> 4;
    b_off[ni] = Cfg::A_BYTES + panel * 8192 + (8 * g + tq) * 128 + ((c32 ^ bf) * 32) + tp * 8;
  }

  f32x4 acc[Cfg::MF][Cfg::NF];
#pragma unroll
  for (int mi = 0; mi < Cfg::MF; ++mi)
#pragma unroll
    for (int ni = 0; ni < Cfg::NF; ++ni) acc[mi][ni] = f32x4{0.f, 0.f, 0.f, 0.f};

  const int nk = K / Cfg::BK;
  stage_load(0);
  stage_load(1);
  int buf = 0;
  for (int kt = 0; kt < nk; ++kt) {
    if (kt + 1 < nk) asm volatile("s_waitcnt vmcnt(%0)" ::"i"(Cfg::LOADS) : "memory");
    else asm volatile("s_waitcnt vmcnt(0)" ::: "memory");
    __builtin_amdgcn_s_barrier();
    if (kt + 2 < nk) stage_load(buf >= 1 ? buf - 1 : 2);
    const char* sb = smem + buf * Cfg::STAGE;
#pragma unroll
    for (int kk = 0; kk < 2; ++kk) {
      bf16x8 af[Cfg::MF], bfr[Cfg::NF];
#pragma unroll
      for (int ni = 0; ni < Cfg::NF; ++ni) {
        const char* p = sb + b_off[ni] + kk * 32 * 128;
        v4i16_t lo = __builtin_amdgcn_ds_read_tr16_b64_v4i16((LAS v4i16_t*)(p));
        v4i16_t hi = __builtin_amdgcn_ds_read_tr16_b64_v4i16((LAS v4i16_t*)(p + 4 * 128));
        bfr[ni] = bf16x8{lo[0], lo[1], lo[2], lo[3], hi[0], hi[1], hi[2], hi[3]};
      }
#pragma unroll
      for (int mi = 0; mi < Cfg::MF; ++mi)
        af[mi] = *reinterpret_cast<const bf16x8*>(sb + a_off[kk] + mi * 16 * 128);
#pragma unroll
      for (int mi = 0; mi < Cfg::MF; ++mi)
#pragma unroll
        for (int ni = 0; ni < Cfg::NF; ++ni)
          acc[mi][ni] = __builtin_amdgcn_mfma_f32_16x16x32_bf16(af[mi], bfr[ni], acc[mi][ni], 0, 0, 0);
    }
    buf = (buf == 2) ? 0 : buf + 1;
  }

  __builtin_amdgcn_s_barrier();
  u16* cs = reinterpret_cast<u16*>(smem);
  constexpr int CLD = BN + Cfg::CPAD;
#pragma unroll
  for (int ni = 0; ni < Cfg::NF; ++ni) {
    const int col = wc * Cfg::WTN + ni * 16 + fr;
    float bv = 0.f;
    if (EPI == EPI_GELU) bv = bf2f(bias[n0 + col]);
#pragma unroll
    for (int mi = 0; mi < Cfg::MF; ++mi) {
#pragma unroll
      for (int r = 0; r < 4; ++r) {
        const int row = wr * 64 + mi * 16 + g * 4 + r;
        float v = rbf(acc[mi][ni][r]);
        if (EPI == EPI_QKV) {
          if (n0 + col < 2 * D_) {
            v = (v > 0.f) ? rbf(v + 1.f) : rbf(__expf(v));
            v = v + 1e-6f;
          }
        } else if (EPI == EPI_GELU) {
          v = gelu_tanh(rbf(v + bv));
        }
        cs[row * CLD + col] = f2bf(v);
      }
    }
  }
  __builtin_amdgcn_s_waitcnt(0xc07f);
  __builtin_amdgcn_s_barrier();
  constexpr int CPR = BN / 8;
  constexpr int NCH = Cfg::BM * CPR;
#pragma unroll
  for (int i = tid; i < NCH; i += Cfg::THREADS) {
    const int row = i / CPR, c8 = (i % CPR) * 8;
    u16x8 v = *reinterpret_cast<const u16x8*>(cs + row * CLD + c8);
    const size_t go = (size_t)(m0 + row) * N + n0 + c8;
    if (EPI == EPI_RES || EPI == EPI_RES_BIAS) {
      u16x8 rv = *reinterpret_cast<const u16x8*>(res + go);
      u16x8 bb;
      if (EPI == EPI_RES_BIAS) bb = *reinterpret_cast<const u16x8*>(bias + n0 + c8);
#pragma unroll
      for (int e = 0; e < 8; ++e) {
        float t = bf2f(v[e]);
        if (EPI == EPI_RES_BIAS) t = rbf(t + bf2f(bb[e]));
        v[e] = f2bf(bf2f(rv[e]) + t);
      }
    }
    *reinterpret_cast<u16x8*>(C + go) = v;
  }
}

__global__ __launch_bounds__(256) void kv_kernel(const u16* __restrict__ qkv, float* __restrict__ part) {
  __shared__ float sk[64][33], sv[64][32];
  const int h = blockIdx.x, ch = blockIdx.y, t = threadIdx.x;
  const int d = t >> 3, e4 = (t & 7) * 4;
  float acc[4] = {0.f, 0.f, 0.f, 0.f}, z = 0.f;
  for (int r0 = 0; r0 < 256; r0 += 64) {
    __syncthreads();
    {
      const int row = t >> 2, c8 = (t & 3) * 8;
      const size_t base = (size_t)(ch * 256 + r0 + row) * (3 * D_) + h * DH_ + c8;
      u16x8 kk = *reinterpret_cast<const u16x8*>(qkv + base + D_);
      u16x8 vv = *reinterpret_cast<const u16x8*>(qkv + base + 2 * D_);
#pragma unroll
      for (int i = 0; i < 8; ++i) { sk[row][c8 + i] = bf2f(kk[i]); sv[row][c8 + i] = bf2f(vv[i]); }
    }
    __syncthreads();
#pragma unroll 8
    for (int r = 0; r < 64; ++r) {
      const float kd = sk[r][d];
      const f32x4 v4 = *reinterpret_cast<const f32x4*>(&sv[r][e4]);
      acc[0] += kd * v4[0]; acc[1] += kd * v4[1]; acc[2] += kd * v4[2]; acc[3] += kd * v4[3];
      z += kd;
    }
  }
  float* p = part + ((size_t)ch * H_ + h) * 1056;
  *reinterpret_cast<f32x4*>(p + d * 32 + e4) = f32x4{acc[0], acc[1], acc[2], acc[3]};
  if ((t & 7) == 0) p[1024 + d] = z;
}

__global__ __launch_bounds__(256) void o_kernel(const u16* __restrict__ qkv, const float* __restrict__ part,
                                                u16* __restrict__ o) {
  __shared__ float skv[32][32], sz[32];
  const int h = blockIdx.x, rb = blockIdx.y, t = threadIdx.x;
  for (int i = t; i < 1056; i += 256) {
    float s = 0.f;
#pragma unroll
    for (int c = 0; c < 8; ++c) s += part[((size_t)c * H_ + h) * 1056 + i];
    s = rbf(s);
    if (i < 1024) skv[i >> 5][i & 31] = s; else sz[i - 1024] = s;
  }
  __syncthreads();
  const int row = rb * 256 + t;
  const u16x8* qp = reinterpret_cast<const u16x8*>(qkv + (size_t)row * (3 * D_) + h * DH_);
  float q[32];
#pragma unroll
  for (int c = 0; c < 4; ++c) {
    u16x8 a = qp[c];
#pragma unroll
    for (int i = 0; i < 8; ++i) q[c * 8 + i] = bf2f(a[i]);
  }
  float den = 0.f;
#pragma unroll
  for (int d = 0; d < 32; ++d) den += q[d] * sz[d];
  den = rbf(rbf(den) + 1e-6f);
  u16x8 ov[4];
#pragma unroll
  for (int e = 0; e < 32; ++e) {
    float num = 0.f;
#pragma unroll
    for (int d = 0; d < 32; ++d) num += q[d] * skv[d][e];
    ov[e >> 3][e & 7] = f2bf(rbf(num) / den);
  }
  u16x8* op = reinterpret_cast<u16x8*>(o + (size_t)row * D_ + h * DH_);
#pragma unroll
  for (int c = 0; c < 4; ++c) op[c] = ov[c];
}

template <int BN, int WN, int EPI>
static void launch_gemm(const u16* A, const u16* B, u16* C, const u16* bias, const u16* res, int N, int K,
                        hipStream_t stream) {
  using Cfg = GemmCfg<BN, WN>;
  static bool attr = false;
  if (!attr) {
    hipFuncSetAttribute((const void*)gemm_kernel<BN, WN, EPI>, hipFuncAttributeMaxDynamicSharedMemorySize, Cfg::LDS_BYTES);
    attr = true;
  }
  const int ntn = N / BN;
  gemm_kernel<BN, WN, EPI><<<(S_ / 128) * ntn, Cfg::THREADS, Cfg::LDS_BYTES, stream>>>(A, B, C, bias, res, N, K, ntn);
}

extern "C" void kernel_launch(void* const* d_in, const int* in_sizes, int n_in, void* d_out, int out_size,
                              void* d_ws, size_t ws_size, hipStream_t stream) {
  const u16* x0 = (const u16*)d_in[0];
  const u16* wqkv = (const u16*)d_in[1];
  const u16* wo = (const u16*)d_in[2];
  const u16* w1 = (const u16*)d_in[4];
  const u16* b1 = (const u16*)d_in[5];
  const u16* w2 = (const u16*)d_in[6];
  const u16* b2 = (const u16*)d_in[7];
  u16* out = (u16*)d_out;
  char* ws = (char*)d_ws;
  u16* xn = (u16*)(ws);
  u16* qkv = (u16*)(ws + (4u << 20));
  u16* ob = (u16*)(ws + (16u << 20));
  u16* xs = (u16*)(ws + (20u << 20));
  u16* hb = (u16*)(ws + (24u << 20));
  float* part = (float*)(ws + (40u << 20));

  for (int layer = 0; layer < 2; ++layer) {
    const u16* xin = layer == 0 ? x0 : xs;
    ln_kernel<0><<<S_ / 4, 256, 0, stream>>>(xin, xn);
    launch_gemm<192, 4, EPI_QKV>(xn, wqkv, qkv, nullptr, nullptr, 3 * D_, D_, stream);
    kv_kernel<<<dim3(H_, 8), 256, 0, stream>>>(qkv, part);
    o_kernel<<<dim3(H_, 8), 256, 0, stream>>>(qkv, part, ob);
    launch_gemm<64, 2, EPI_RES>(ob, wo, xs, nullptr, xin, D_, D_, stream);
    ln_kernel<1><<<S_ / 4, 256, 0, stream>>>(xs, xn);
    launch_gemm<256, 4, EPI_GELU>(xn, w1, hb, b1, nullptr, F_, D_, stream);
    launch_gemm<64, 2, EPI_RES_BIAS>(hb, w2, layer == 1 ? out : xs, b2, xs, D_, F_, stream);
  }
}
```

```cpp
#include <hip/hip_runtime.h>
#include <hip/hip_bf16.h>
#include <stdint.h>

typedef unsigned short u16;
using bf16x8 = __attribute__((ext_vector_type(8))) short;
using s16x4  = __attribute__((ext_vector_type(4))) short;
using f32x4  = __attribute__((ext_vector_type(4))) float;
using u16x8  = __attribute__((ext_vector_type(8))) unsigned short;
typedef short v4i16_t __attribute__((ext_vector_type(4)));
#define LAS __attribute__((address_space(3)))

static constexpr int S_ = 2048, D_ = 1024, H_ = 32, DH_ = 32, F_ = 3584;

__device__ __forceinline__ float bf2f(u16 u) { return __uint_as_float(((unsigned)u) << 16); }
__device__ __forceinline__ u16 f2bf(float f) {
  unsigned u = __float_as_uint(f);
  u += 0x7fffu + ((u >> 16) & 1u);
  return (u16)(u >> 16);
}
__device__ __forceinline__ float rbf(float f) { return bf2f(f2bf(f)); }
__device__ __forceinline__ unsigned pk2(float a, float b) {
  unsigned r; asm("v_cvt_pk_bf16_f32 %0, %1, %2" : "=v"(r) : "v"(a), "v"(b)); return r;
}
using u32x2 = __attribute__((ext_vector_type(2))) unsigned;

template <int TWICE>
__global__ __launch_bounds__(256) void ln_kernel(const u16* __restrict__ x, u16* __restrict__ y) {
  const int row = blockIdx.x * 4 + (threadIdx.x >> 6);
  const int lane = threadIdx.x & 63;
  const u16x8* xp = reinterpret_cast<const u16x8*>(x + (size_t)row * D_);
  u16x8 a = xp[lane], b = xp[lane + 64];
  float v[16];
#pragma unroll
  for (int i = 0; i < 8; ++i) { v[i] = bf2f(a[i]); v[8 + i] = bf2f(b[i]); }
#pragma unroll
  for (int pass = 0; pass < (TWICE ? 2 : 1); ++pass) {
    float s = 0.f;
#pragma unroll
    for (int i = 0; i < 16; ++i) s += v[i];
#pragma unroll
    for (int o = 32; o >= 1; o >>= 1) s += __shfl_xor(s, o);
    const float m = s * (1.f / D_);
    float q = 0.f;
#pragma unroll
    for (int i = 0; i < 16; ++i) { float d = v[i] - m; q += d * d; }
#pragma unroll
    for (int o = 32; o >= 1; o >>= 1) q += __shfl_xor(q, o);
    const float r = rsqrtf(q * (1.f / D_) + 1e-5f);
#pragma unroll
    for (int i = 0; i < 16; ++i) v[i] = rbf((v[i] - m) * r);
  }
  u16x8 oa, ob;
#pragma unroll
  for (int i = 0; i < 8; ++i) { oa[i] = f2bf(v[i]); ob[i] = f2bf(v[8 + i]); }
  u16x8* yp = reinterpret_cast<u16x8*>(y + (size_t)row * D_);
  yp[lane] = oa; yp[lane + 64] = ob;
}

enum { EPI_QKV = 0, EPI_GELU = 1, EPI_RES = 2, EPI_RES_BIAS = 3 };

template <int BN, int WN>
struct GemmCfg {
  static constexpr int BM = 128, BK = 64, NST = 3;
  static constexpr int THREADS = 128 * WN;
  static constexpr int NWAVES = 2 * WN;
  static constexpr int A_BYTES = BM * BK * 2;
  static constexpr int B_BYTES = BN * BK * 2;
  static constexpr int STAGE = A_BYTES + B_BYTES;
  static constexpr int A_PIECES = A_BYTES / 1024, B_PIECES = B_BYTES / 1024;
  static constexpr int A_PW = A_PIECES / NWAVES, B_PW = B_PIECES / NWAVES;
  static constexpr int LOADS = A_PW + B_PW;
  static constexpr int WTN = BN / WN;
  static constexpr int MF = 4, NF = WTN / 16;
  static constexpr int CPAD = 8;
  static constexpr int C_BYTES = BM * (BN + CPAD) * 2;
  static constexpr int LDS_BYTES = (NST * STAGE > C_BYTES) ? NST * STAGE : C_BYTES;
};

__device__ __forceinline__ void glds16(const void* g, unsigned lds_base) {
  unsigned sv;
  asm volatile("s_mov_b32 %0, m0\n\ts_mov_b32 m0, %2\n\ts_nop 0\n\tglobal_load_lds_dwordx4 %1, off\n\ts_mov_b32 m0, %0"
               : "=&s"(sv) : "v"(g), "s"(lds_base) : "memory");
}

__device__ __forceinline__ int bswz(int k) { return ((k >> 1) & 1) | (((k >> 3) & 1) << 1); }

__device__ __forceinline__ float gelu_tanh(float x) {
  const float u = 0.7978845608028654f * (x + 0.044715f * x * x * x);
  const float e = __expf(2.f * u);
  const float t = 1.f - 2.f / (e + 1.f);
  return 0.5f * x * (1.f + t);
}

template <int BN, int WN, int EPI>
__global__ __launch_bounds__(128 * WN) void gemm_kernel(const u16* __restrict__ A, const u16* __restrict__ B,
                                                         u16* __restrict__ C, const u16* __restrict__ bias,
                                                         const u16* __restrict__ res, float* __restrict__ part,
                                                         int N, int K, int ntn) {
  using Cfg = GemmCfg<BN, WN>;
  extern __shared__ __attribute__((aligned(16))) char smem[];
  const int tid = threadIdx.x, lane = tid & 63, wid = __builtin_amdgcn_readfirstlane(tid >> 6);
  const int wr = wid / WN, wc = wid % WN;
  const unsigned smem_base = (unsigned)(size_t)(LAS char*)smem;
  const int bm = blockIdx.x / ntn, bn = blockIdx.x % ntn;
  const int m0 = bm * Cfg::BM, n0 = bn * BN;

  const char* asrc[Cfg::A_PW];
  const char* bsrc[Cfg::B_PW];
#pragma unroll
  for (int j = 0; j < Cfg::A_PW; ++j) {
    const int p = wid * Cfg::A_PW + j;
    const int row = p * 8 + (lane >> 3), slot = lane & 7;
    const int chunk = slot ^ (row & 7);
    asrc[j] = reinterpret_cast<const char*>(A + (size_t)(m0 + row) * K + chunk * 8);
  }
#pragma unroll
  for (int j = 0; j < Cfg::B_PW; ++j) {
    const int q = wid * Cfg::B_PW + j;
    const int panel = q >> 3, kr = (q & 7) * 8 + (lane >> 3), s = lane & 7;
    const int c32 = (s >> 1) ^ bswz(kr);
    const int tc = panel * 64 + c32 * 16 + (s & 1) * 8;
    int gc = n0 + tc;
    if (EPI == EPI_QKV) gc = ((tc >> 5) % 3) * D_ + (2 * bn + tc / 96) * DH_ + (tc & 31);
    bsrc[j] = reinterpret_cast<const char*>(B + (size_t)kr * N + gc);
  }
  const size_t a_step = (size_t)Cfg::BK * 2;
  const size_t b_step = (size_t)Cfg::BK * N * 2;

  auto stage_load = [&](int buf) {
    const unsigned base = smem_base + buf * Cfg::STAGE;
#pragma unroll
    for (int j = 0; j < Cfg::A_PW; ++j) {
      glds16(asrc[j], base + (wid * Cfg::A_PW + j) * 1024);
      asrc[j] += a_step;
    }
#pragma unroll
    for (int j = 0; j < Cfg::B_PW; ++j) {
      glds16(bsrc[j], base + Cfg::A_BYTES + (wid * Cfg::B_PW + j) * 1024);
      bsrc[j] += b_step;
    }
  };

  const int g = lane >> 4, fr = lane & 15;
  int a_off[2];
#pragma unroll
  for (int kk = 0; kk < 2; ++kk) a_off[kk] = (wr * 64 + fr) * 128 + (((kk * 4 + g) ^ (fr & 7)) * 16);
  const int tq = fr >> 2, tp = fr & 3;
  const int bf = (tq >> 1) | ((g & 1) << 1);
  int b_off[Cfg::NF];
#pragma unroll
  for (int ni = 0; ni < Cfg::NF; ++ni) {
    const int col0 = wc * Cfg::WTN + ni * 16;
    const int panel = col0 >> 6, c32 = (col0 & 63) >> 4;
    b_off[ni] = Cfg::A_BYTES + panel * 8192 + (8 * g + tq) * 128 + ((c32 ^ bf) * 32) + tp * 8;
  }

  f32x4 acc[Cfg::MF][Cfg::NF];
#pragma unroll
  for (int mi = 0; mi < Cfg::MF; ++mi)
#pragma unroll
    for (int ni = 0; ni < Cfg::NF; ++ni) acc[mi][ni] = f32x4{0.f, 0.f, 0.f, 0.f};

  const int nk = K / Cfg::BK;
  stage_load(0);
  stage_load(1);
  int buf = 0;
  for (int kt = 0; kt < nk; ++kt) {
    if (kt + 1 < nk) asm volatile("s_waitcnt vmcnt(%0)" ::"i"(Cfg::LOADS) : "memory");
    else asm volatile("s_waitcnt vmcnt(0)" ::: "memory");
    __builtin_amdgcn_s_barrier();
    if (kt + 2 < nk) stage_load(buf >= 1 ? buf - 1 : 2);
    const char* sb = smem + buf * Cfg::STAGE;
#pragma unroll
    for (int kk = 0; kk < 2; ++kk) {
      bf16x8 af[Cfg::MF], bfr[Cfg::NF];
#pragma unroll
      for (int ni = 0; ni < Cfg::NF; ++ni) {
        const char* p = sb + b_off[ni] + kk * 32 * 128;
        v4i16_t lo = __builtin_amdgcn_ds_read_tr16_b64_v4i16((LAS v4i16_t*)(p));
        v4i16_t hi = __builtin_amdgcn_ds_read_tr16_b64_v4i16((LAS v4i16_t*)(p + 4 * 128));
        bfr[ni] = bf16x8{lo[0], lo[1], lo[2], lo[3], hi[0], hi[1], hi[2], hi[3]};
      }
#pragma unroll
      for (int mi = 0; mi < Cfg::MF; ++mi)
        af[mi] = *reinterpret_cast<const bf16x8*>(sb + a_off[kk] + mi * 16 * 128);
#pragma unroll
      for (int mi = 0; mi < Cfg::MF; ++mi)
#pragma unroll
        for (int ni = 0; ni < Cfg::NF; ++ni)
          acc[mi][ni] = __builtin_amdgcn_mfma_f32_16x16x32_bf16(bfr[ni], af[mi], acc[mi][ni], 0, 0, 0);
    }
    buf = (buf == 2) ? 0 : buf + 1;
  }

  __builtin_amdgcn_s_barrier();
  u16* cs = reinterpret_cast<u16*>(smem);
  constexpr int CLD = BN + Cfg::CPAD;
#pragma unroll
  for (int ni = 0; ni < Cfg::NF; ++ni) {
    const int col = wc * Cfg::WTN + ni * 16 + 4 * g;
    float bv[4] = {0.f, 0.f, 0.f, 0.f};
    if (EPI == EPI_GELU) {
      const u32x2 bb = *reinterpret_cast<const u32x2*>(bias + n0 + col);
      bv[0] = __uint_as_float(bb[0] << 16); bv[1] = __uint_as_float(bb[0] & 0xffff0000u);
      bv[2] = __uint_as_float(bb[1] << 16); bv[3] = __uint_as_float(bb[1] & 0xffff0000u);
    }
    const bool isqk = (EPI == EPI_QKV) && ((col >> 5) % 3) < 2;
#pragma unroll
    for (int mi = 0; mi < Cfg::MF; ++mi) {
      const int row = wr * 64 + mi * 16 + fr;
      float v[4];
#pragma unroll
      for (int r = 0; r < 4; ++r) {
        float t = acc[mi][ni][r];
        if (EPI == EPI_QKV) {
          const float ph = (t > 0.f ? t + 1.f : __expf(t)) + 1e-6f;
          t = isqk ? ph : t;
        } else if (EPI == EPI_GELU) {
          t = gelu_tanh(t + bv[r]);
        }
        v[r] = t;
      }
      *reinterpret_cast<u32x2*>(cs + row * CLD + col) = u32x2{pk2(v[0], v[1]), pk2(v[2], v[3])};
    }
  }
  __builtin_amdgcn_s_waitcnt(0xc07f);
  __builtin_amdgcn_s_barrier();
  if (EPI == EPI_QKV) {
    for (int i = tid; i < Cfg::BM * 8; i += Cfg::THREADS) {
      const int row = i >> 3, c = i & 7;
      const u16x8 v = *reinterpret_cast<const u16x8*>(cs + row * CLD + (c >> 2) * 96 + (c & 3) * 8);
      *reinterpret_cast<u16x8*>(C + (size_t)(m0 + row) * D_ + bn * 64 + c * 8) = v;
    }
    const int hh = wid >> 2, dt = (wid >> 1) & 1, et = wid & 1;
    f32x4 kv = {0.f, 0.f, 0.f, 0.f}, zz = {0.f, 0.f, 0.f, 0.f};
    const bf16x8 ones = {0x3f80, 0x3f80, 0x3f80, 0x3f80, 0x3f80, 0x3f80, 0x3f80, 0x3f80};
#pragma unroll
    for (int ks = 0; ks < 4; ++ks) {
      const int srow = ks * 32 + 8 * g + tq;
      const char* pk = reinterpret_cast<const char*>(cs + srow * CLD + hh * 96 + 32 + dt * 16 + tp * 4);
      const char* pv = reinterpret_cast<const char*>(cs + srow * CLD + hh * 96 + 64 + et * 16 + tp * 4);
      v4i16_t k0 = __builtin_amdgcn_ds_read_tr16_b64_v4i16((LAS v4i16_t*)(pk));
      v4i16_t k1 = __builtin_amdgcn_ds_read_tr16_b64_v4i16((LAS v4i16_t*)(pk + 4 * CLD * 2));
      v4i16_t v0 = __builtin_amdgcn_ds_read_tr16_b64_v4i16((LAS v4i16_t*)(pv));
      v4i16_t v1 = __builtin_amdgcn_ds_read_tr16_b64_v4i16((LAS v4i16_t*)(pv + 4 * CLD * 2));
      const bf16x8 kf = {k0[0], k0[1], k0[2], k0[3], k1[0], k1[1], k1[2], k1[3]};
      const bf16x8 vf = {v0[0], v0[1], v0[2], v0[3], v1[0], v1[1], v1[2], v1[3]};
      kv = __builtin_amdgcn_mfma_f32_16x16x32_bf16(kf, vf, kv, 0, 0, 0);
      zz = __builtin_amdgcn_mfma_f32_16x16x32_bf16(kf, ones, zz, 0, 0, 0);
    }
    float* pp = part + ((size_t)bm * H_ + 2 * bn + hh) * 1056;
#pragma unroll
    for (int r = 0; r < 4; ++r) pp[(dt * 16 + 4 * g + r) * 32 + et * 16 + fr] = kv[r];
    if (et == 0 && fr == 0) {
#pragma unroll
      for (int r = 0; r < 4; ++r) pp[1024 + dt * 16 + 4 * g + r] = zz[r];
    }
    return;
  }
  constexpr int CPR = BN / 8;
  constexpr int NCH = Cfg::BM * CPR;
#pragma unroll
  for (int i = tid; i < NCH; i += Cfg::THREADS) {
    const int row = i / CPR, c8 = (i % CPR) * 8;
    u16x8 v = *reinterpret_cast<const u16x8*>(cs + row * CLD + c8);
    const size_t go = (size_t)(m0 + row) * N + n0 + c8;
    if (EPI == EPI_RES || EPI == EPI_RES_BIAS) {
      u16x8 rv = *reinterpret_cast<const u16x8*>(res + go);
      u16x8 bb;
      if (EPI == EPI_RES_BIAS) bb = *reinterpret_cast<const u16x8*>(bias + n0 + c8);
#pragma unroll
      for (int e = 0; e < 8; e += 2) {
        float t0 = bf2f(v[e]), t1 = bf2f(v[e + 1]);
        if (EPI == EPI_RES_BIAS) { t0 += bf2f(bb[e]); t1 += bf2f(bb[e + 1]); }
        const unsigned pk = pk2(bf2f(rv[e]) + t0, bf2f(rv[e + 1]) + t1);
        v[e] = (u16)(pk & 0xffffu); v[e + 1] = (u16)(pk >> 16);
      }
    }
    *reinterpret_cast<u16x8*>(C + go) = v;
  }
}

__global__ __launch_bounds__(256) void o_kernel(const u16* __restrict__ pq, const float* __restrict__ part,
                                                u16* __restrict__ o) {
  __shared__ __attribute__((aligned(16))) float skv[1056];
  const int h = blockIdx.x, rb = blockIdx.y, t = threadIdx.x;
  for (int i = t; i < 1056; i += 256) {
    float s = 0.f;
#pragma unroll
    for (int c = 0; c < 16; ++c) s += part[((size_t)c * H_ + h) * 1056 + i];
    skv[i] = s;
  }
  __syncthreads();
  const int row = rb * 128 + (t >> 1), eh = (t & 1) * 16;
  const u16x8* qp = reinterpret_cast<const u16x8*>(pq + (size_t)row * D_ + h * DH_);
  float q[32];
#pragma unroll
  for (int c = 0; c < 4; ++c) {
    u16x8 a = qp[c];
#pragma unroll
    for (int i = 0; i < 8; ++i) q[c * 8 + i] = bf2f(a[i]);
  }
  float den = 1e-6f;
#pragma unroll
  for (int d = 0; d < 32; ++d) den += q[d] * skv[1024 + d];
  const float inv = 1.f / den;
  float num[16];
#pragma unroll
  for (int e = 0; e < 16; ++e) num[e] = 0.f;
#pragma unroll
  for (int d = 0; d < 32; ++d) {
#pragma unroll
    for (int e4 = 0; e4 < 4; ++e4) {
      const f32x4 kk = *reinterpret_cast<const f32x4*>(&skv[d * 32 + eh + e4 * 4]);
      num[e4 * 4 + 0] += q[d] * kk[0]; num[e4 * 4 + 1] += q[d] * kk[1];
      num[e4 * 4 + 2] += q[d] * kk[2]; num[e4 * 4 + 3] += q[d] * kk[3];
    }
  }
  u16x8 ov[2];
#pragma unroll
  for (int e = 0; e < 16; e += 2) {
    const unsigned pk = pk2(num[e] * inv, num[e + 1] * inv);
    ov[e >> 3][e & 7] = (u16)(pk & 0xffffu); ov[e >> 3][(e & 7) + 1] = (u16)(pk >> 16);
  }
  u16x8* op = reinterpret_cast<u16x8*>(o + (size_t)row * D_ + h * DH_ + eh);
  op[0] = ov[0]; op[1] = ov[1];
}

template <int BN, int WN, int EPI>
static void launch_gemm(const u16* A, const u16* B, u16* C, const u16* bias, const u16* res, float* part, int N,
                        int K, hipStream_t stream) {
  using Cfg = GemmCfg<BN, WN>;
  (void)hipFuncSetAttribute((const void*)gemm_kernel<BN, WN, EPI>, hipFuncAttributeMaxDynamicSharedMemorySize, Cfg::LDS_BYTES);
  const int ntn = N / BN;
  gemm_kernel<BN, WN, EPI><<<(S_ / 128) * ntn, Cfg::THREADS, Cfg::LDS_BYTES, stream>>>(A, B, C, bias, res, part, N, K, ntn);
}

extern "C" void kernel_launch(void* const* d_in, const int* in_sizes, int n_in, void* d_out, int out_size,
                              void* d_ws, size_t ws_size, hipStream_t stream) {
  const u16* x0 = (const u16*)d_in[0];
  const u16* wqkv = (const u16*)d_in[1];
  const u16* wo = (const u16*)d_in[2];
  const u16* w1 = (const u16*)d_in[4];
  const u16* b1 = (const u16*)d_in[5];
  const u16* w2 = (const u16*)d_in[6];
  const u16* b2 = (const u16*)d_in[7];
  u16* out = (u16*)d_out;
  char* ws = (char*)d_ws;
  u16* xn = (u16*)(ws);
  u16* qkv = (u16*)(ws + (4u << 20));
  u16* ob = (u16*)(ws + (16u << 20));
  u16* xs = (u16*)(ws + (20u << 20));
  u16* hb = (u16*)(ws + (24u << 20));
  float* part = (float*)(ws + (40u << 20));

  for (int layer = 0; layer < 2; ++layer) {
    const u16* xin = layer == 0 ? x0 : xs;
    ln_kernel<0><<<S_ / 4, 256, 0, stream>>>(xin, xn);
    launch_gemm<192, 4, EPI_QKV>(xn, wqkv, qkv, nullptr, nullptr, part, 3 * D_, D_, stream);
    o_kernel<<<dim3(H_, 16), 256, 0, stream>>>(qkv, part, ob);
    launch_gemm<64, 2, EPI_RES>(ob, wo, xs, nullptr, xin, nullptr, D_, D_, stream);
    ln_kernel<1><<<S_ / 4, 256, 0, stream>>>(xs, xn);
    launch_gemm<256, 4, EPI_GELU>(xn, w1, hb, b1, nullptr, nullptr, F_, D_, stream);
    launch_gemm<64, 2, EPI_RES_BIAS>(hb, w2, layer == 1 ? out : xs, b2, xs, nullptr, D_, F_, stream);
  }
}
```

```cpp
#include <hip/hip_runtime.h>
#include <hip/hip_bf16.h>
#include <stdint.h>

typedef unsigned short u16;
using bf16x8 = __attribute__((ext_vector_type(8))) short;
using s16x4  = __attribute__((ext_vector_type(4))) short;
using f32x4  = __attribute__((ext_vector_type(4))) float;
using u16x8  = __attribute__((ext_vector_type(8))) unsigned short;
typedef short v4i16_t __attribute__((ext_vector_type(4)));
#define LAS __attribute__((address_space(3)))

static constexpr int S_ = 2048, D_ = 1024, H_ = 32, DH_ = 32, F_ = 3584;

__device__ __forceinline__ float bf2f(u16 u) { return __uint_as_float(((unsigned)u) << 16); }
__device__ __forceinline__ u16 f2bf(float f) {
  unsigned u = __float_as_uint(f);
  u += 0x7fffu + ((u >> 16) & 1u);
  return (u16)(u >> 16);
}
__device__ __forceinline__ float rbf(float f) { return bf2f(f2bf(f)); }
__device__ __forceinline__ unsigned pk2(float a, float b) {
  unsigned r; asm("v_cvt_pk_bf16_f32 %0, %1, %2" : "=v"(r) : "v"(a), "v"(b)); return r;
}
using u32x2 = __attribute__((ext_vector_type(2))) unsigned;

template <int TWICE, int COMBINE, int NORM>
__global__ __launch_bounds__(256) void ln_kernel(const u16* __restrict__ x, u16* __restrict__ y,
                                                 const float* __restrict__ part, const u16* __restrict__ bias,
                                                 u16* __restrict__ xo) {
  const int row = blockIdx.x * 4 + (threadIdx.x >> 6);
  const int lane = threadIdx.x & 63;
  const u16x8* xp = reinterpret_cast<const u16x8*>(x + (size_t)row * D_);
  u16x8 a = xp[2 * lane], b = xp[2 * lane + 1];
  float v[16];
#pragma unroll
  for (int i = 0; i < 8; ++i) { v[i] = bf2f(a[i]); v[8 + i] = bf2f(b[i]); }
  if (COMBINE) {
    float t[16];
    const u16x8 b0 = reinterpret_cast<const u16x8*>(bias)[2 * lane], b1 = reinterpret_cast<const u16x8*>(bias)[2 * lane + 1];
#pragma unroll
    for (int i = 0; i < 8; ++i) { t[i] = bf2f(b0[i]); t[8 + i] = bf2f(b1[i]); }
#pragma unroll
    for (int sp = 0; sp < 4; ++sp) {
      const f32x4* pp = reinterpret_cast<const f32x4*>(part + (size_t)sp * S_ * D_ + (size_t)row * D_ + lane * 16);
#pragma unroll
      for (int q = 0; q < 4; ++q) { const f32x4 u = pp[q]; t[4 * q] += u[0]; t[4 * q + 1] += u[1]; t[4 * q + 2] += u[2]; t[4 * q + 3] += u[3]; }
    }
    u16x8 oa, ob;
#pragma unroll
    for (int i = 0; i < 16; ++i) v[i] = rbf(v[i] + t[i]);
#pragma unroll
    for (int i = 0; i < 8; ++i) { oa[i] = f2bf(v[i]); ob[i] = f2bf(v[8 + i]); }
    u16x8* op = reinterpret_cast<u16x8*>(xo + (size_t)row * D_);
    op[2 * lane] = oa; op[2 * lane + 1] = ob;
  }
  if (!NORM) return;
#pragma unroll
  for (int pass = 0; pass < (TWICE ? 2 : 1); ++pass) {
    float s = 0.f;
#pragma unroll
    for (int i = 0; i < 16; ++i) s += v[i];
#pragma unroll
    for (int o = 32; o >= 1; o >>= 1) s += __shfl_xor(s, o);
    const float m = s * (1.f / D_);
    float q = 0.f;
#pragma unroll
    for (int i = 0; i < 16; ++i) { float d = v[i] - m; q += d * d; }
#pragma unroll
    for (int o = 32; o >= 1; o >>= 1) q += __shfl_xor(q, o);
    const float r = rsqrtf(q * (1.f / D_) + 1e-5f);
#pragma unroll
    for (int i = 0; i < 16; ++i) v[i] = rbf((v[i] - m) * r);
  }
  u16x8 oa, ob;
#pragma unroll
  for (int i = 0; i < 8; ++i) { oa[i] = f2bf(v[i]); ob[i] = f2bf(v[8 + i]); }
  u16x8* yp = reinterpret_cast<u16x8*>(y + (size_t)row * D_);
  yp[2 * lane] = oa; yp[2 * lane + 1] = ob;
}

enum { EPI_QKV = 0, EPI_GELU = 1, EPI_RES = 2, EPI_PART = 4 };

template <int BN, int WN>
struct GemmCfg {
  static constexpr int BM = 128, BK = 64, NST = 3;
  static constexpr int THREADS = 128 * WN;
  static constexpr int NWAVES = 2 * WN;
  static constexpr int A_BYTES = BM * BK * 2;
  static constexpr int B_BYTES = BN * BK * 2;
  static constexpr int STAGE = A_BYTES + B_BYTES;
  static constexpr int A_PIECES = A_BYTES / 1024, B_PIECES = B_BYTES / 1024;
  static constexpr int A_PW = A_PIECES / NWAVES, B_PW = B_PIECES / NWAVES;
  static constexpr int LOADS = A_PW + B_PW;
  static constexpr int WTN = BN / WN;
  static constexpr int MF = 4, NF = WTN / 16;
  static constexpr int CPAD = 8;
  static constexpr int C_BYTES = BM * (BN + CPAD) * 2;
  static constexpr int LDS_BYTES = (NST * STAGE > C_BYTES) ? NST * STAGE : C_BYTES;
};

__device__ __forceinline__ void glds16(const void* g, unsigned lds_base) {
  unsigned sv;
  asm volatile("s_mov_b32 %0, m0\n\ts_mov_b32 m0, %2\n\ts_nop 0\n\tglobal_load_lds_dwordx4 %1, off\n\ts_mov_b32 m0, %0"
               : "=&s"(sv) : "v"(g), "s"(lds_base) : "memory");
}

__device__ __forceinline__ int bswz(int k) { return ((k >> 1) & 1) | (((k >> 3) & 1) << 1); }

__device__ __forceinline__ float gelu_tanh(float x) {
  const float u = 0.7978845608028654f * (x + 0.044715f * x * x * x);
  const float e = __expf(2.f * u);
  const float t = 1.f - 2.f / (e + 1.f);
  return 0.5f * x * (1.f + t);
}

template <int BN, int WN, int EPI>
__global__ __launch_bounds__(128 * WN) void gemm_kernel(const u16* __restrict__ A, const u16* __restrict__ B,
                                                         u16* __restrict__ C, const u16* __restrict__ bias,
                                                         const u16* __restrict__ res, float* __restrict__ part,
                                                         int N, int K, int ntn) {
  using Cfg = GemmCfg<BN, WN>;
  extern __shared__ __attribute__((aligned(16))) char smem[];
  const int tid = threadIdx.x, lane = tid & 63, wid = __builtin_amdgcn_readfirstlane(tid >> 6);
  const int wr = wid / WN, wc = wid % WN;
  const unsigned smem_base = (unsigned)(size_t)(LAS char*)smem;
  const int split = (EPI == EPI_PART) ? blockIdx.x / (16 * ntn) : 0;
  const int tix = (EPI == EPI_PART) ? blockIdx.x % (16 * ntn) : blockIdx.x;
  const int bm = tix / ntn, bn = tix % ntn;
  const int Kc = (EPI == EPI_PART) ? K / 4 : K;
  A += split * Kc;
  B += (size_t)split * Kc * N;
  const int m0 = bm * Cfg::BM, n0 = bn * BN;

  const char* asrc[Cfg::A_PW];
  const char* bsrc[Cfg::B_PW];
#pragma unroll
  for (int j = 0; j < Cfg::A_PW; ++j) {
    const int p = wid * Cfg::A_PW + j;
    const int row = p * 8 + (lane >> 3), slot = lane & 7;
    const int chunk = slot ^ (row & 7);
    asrc[j] = reinterpret_cast<const char*>(A + (size_t)(m0 + row) * K + chunk * 8);
  }
#pragma unroll
  for (int j = 0; j < Cfg::B_PW; ++j) {
    const int q = wid * Cfg::B_PW + j;
    const int panel = q >> 3, kr = (q & 7) * 8 + (lane >> 3), s = lane & 7;
    const int c32 = (s >> 1) ^ bswz(kr);
    const int tc = panel * 64 + c32 * 16 + (s & 1) * 8;
    int gc = n0 + tc;
    if (EPI == EPI_QKV) gc = ((tc >> 5) % 3) * D_ + (2 * bn + tc / 96) * DH_ + (tc & 31);
    bsrc[j] = reinterpret_cast<const char*>(B + (size_t)kr * N + gc);
  }
  const size_t a_step = (size_t)Cfg::BK * 2;
  const size_t b_step = (size_t)Cfg::BK * N * 2;

  auto stage_load = [&](int buf) {
    const unsigned base = smem_base + buf * Cfg::STAGE;
#pragma unroll
    for (int j = 0; j < Cfg::A_PW; ++j) {
      glds16(asrc[j], base + (wid * Cfg::A_PW + j) * 1024);
      asrc[j] += a_step;
    }
#pragma unroll
    for (int j = 0; j < Cfg::B_PW; ++j) {
      glds16(bsrc[j], base + Cfg::A_BYTES + (wid * Cfg::B_PW + j) * 1024);
      bsrc[j] += b_step;
    }
  };

  const int g = lane >> 4, fr = lane & 15;
  int a_off[2];
#pragma unroll
  for (int kk = 0; kk < 2; ++kk) a_off[kk] = (wr * 64 + fr) * 128 + (((kk * 4 + g) ^ (fr & 7)) * 16);
  const int tq = fr >> 2, tp = fr & 3;
  const int bf = (tq >> 1) | ((g & 1) << 1);
  int b_off[Cfg::NF];
#pragma unroll
  for (int ni = 0; ni < Cfg::NF; ++ni) {
    const int col0 = wc * Cfg::WTN + ni * 16;
    const int panel = col0 >> 6, c32 = (col0 & 63) >> 4;
    b_off[ni] = Cfg::A_BYTES + panel * 8192 + (8 * g + tq) * 128 + ((c32 ^ bf) * 32) + tp * 8;
  }

  f32x4 acc[Cfg::MF][Cfg::NF];
#pragma unroll
  for (int mi = 0; mi < Cfg::MF; ++mi)
#pragma unroll
    for (int ni = 0; ni < Cfg::NF; ++ni) acc[mi][ni] = f32x4{0.f, 0.f, 0.f, 0.f};

  const int nk = Kc / Cfg::BK;
  stage_load(0);
  stage_load(1);
  int buf = 0;
  for (int kt = 0; kt < nk; ++kt) {
    if (kt + 1 < nk) asm volatile("s_waitcnt vmcnt(%0)" ::"i"(Cfg::LOADS) : "memory");
    else asm volatile("s_waitcnt vmcnt(0)" ::: "memory");
    __builtin_amdgcn_s_barrier();
    if (kt + 2 < nk) stage_load(buf >= 1 ? buf - 1 : 2);
    const char* sb = smem + buf * Cfg::STAGE;
#pragma unroll
    for (int kk = 0; kk < 2; ++kk) {
      bf16x8 af[Cfg::MF], bfr[Cfg::NF];
#pragma unroll
      for (int ni = 0; ni < Cfg::NF; ++ni) {
        const char* p = sb + b_off[ni] + kk * 32 * 128;
        v4i16_t lo = __builtin_amdgcn_ds_read_tr16_b64_v4i16((LAS v4i16_t*)(p));
        v4i16_t hi = __builtin_amdgcn_ds_read_tr16_b64_v4i16((LAS v4i16_t*)(p + 4 * 128));
        bfr[ni] = bf16x8{lo[0], lo[1], lo[2], lo[3], hi[0], hi[1], hi[2], hi[3]};
      }
#pragma unroll
      for (int mi = 0; mi < Cfg::MF; ++mi)
        af[mi] = *reinterpret_cast<const bf16x8*>(sb + a_off[kk] + mi * 16 * 128);
#pragma unroll
      for (int mi = 0; mi < Cfg::MF; ++mi)
#pragma unroll
        for (int ni = 0; ni < Cfg::NF; ++ni)
          acc[mi][ni] = __builtin_amdgcn_mfma_f32_16x16x32_bf16(bfr[ni], af[mi], acc[mi][ni], 0, 0, 0);
    }
    buf = (buf == 2) ? 0 : buf + 1;
  }
  if (EPI == EPI_PART) {
    float* pp = part + (size_t)split * S_ * N;
#pragma unroll
    for (int mi = 0; mi < Cfg::MF; ++mi)
#pragma unroll
      for (int ni = 0; ni < Cfg::NF; ++ni)
        *reinterpret_cast<f32x4*>(pp + (size_t)(m0 + wr * 64 + mi * 16 + fr) * N + n0 + wc * Cfg::WTN + ni * 16 + 4 * g) = acc[mi][ni];
    return;
  }

  __builtin_amdgcn_s_barrier();
  u16* cs = reinterpret_cast<u16*>(smem);
  constexpr int CLD = BN + Cfg::CPAD;
#pragma unroll
  for (int ni = 0; ni < Cfg::NF; ++ni) {
    const int col = wc * Cfg::WTN + ni * 16 + 4 * g;
    float bv[4] = {0.f, 0.f, 0.f, 0.f};
    if (EPI == EPI_GELU) {
      const u32x2 bb = *reinterpret_cast<const u32x2*>(bias + n0 + col);
      bv[0] = __uint_as_float(bb[0] << 16); bv[1] = __uint_as_float(bb[0] & 0xffff0000u);
      bv[2] = __uint_as_float(bb[1] << 16); bv[3] = __uint_as_float(bb[1] & 0xffff0000u);
    }
    const bool isqk = (EPI == EPI_QKV) && ((col >> 5) % 3) < 2;
#pragma unroll
    for (int mi = 0; mi < Cfg::MF; ++mi) {
      const int row = wr * 64 + mi * 16 + fr;
      float v[4];
#pragma unroll
      for (int r = 0; r < 4; ++r) {
        float t = acc[mi][ni][r];
        if (EPI == EPI_QKV) {
          const float ph = (t > 0.f ? t + 1.f : __expf(t)) + 1e-6f;
          t = isqk ? ph : t;
        } else if (EPI == EPI_GELU) {
          t = gelu_tanh(t + bv[r]);
        }
        v[r] = t;
      }
      *reinterpret_cast<u32x2*>(cs + row * CLD + col) = u32x2{pk2(v[0], v[1]), pk2(v[2], v[3])};
    }
  }
  __builtin_amdgcn_s_waitcnt(0xc07f);
  __builtin_amdgcn_s_barrier();
  if (EPI == EPI_QKV) {
    for (int i = tid; i < Cfg::BM * 8; i += Cfg::THREADS) {
      const int row = i >> 3, c = i & 7;
      const u16x8 v = *reinterpret_cast<const u16x8*>(cs + row * CLD + (c >> 2) * 96 + (c & 3) * 8);
      *reinterpret_cast<u16x8*>(C + (size_t)(m0 + row) * D_ + bn * 64 + c * 8) = v;
    }
    const int hh = wid >> 2, dt = (wid >> 1) & 1, et = wid & 1;
    f32x4 kv = {0.f, 0.f, 0.f, 0.f}, zz = {0.f, 0.f, 0.f, 0.f};
    const bf16x8 ones = {0x3f80, 0x3f80, 0x3f80, 0x3f80, 0x3f80, 0x3f80, 0x3f80, 0x3f80};
#pragma unroll
    for (int ks = 0; ks < 4; ++ks) {
      const int srow = ks * 32 + 8 * g + tq;
      const char* pk = reinterpret_cast<const char*>(cs + srow * CLD + hh * 96 + 32 + dt * 16 + tp * 4);
      const char* pv = reinterpret_cast<const char*>(cs + srow * CLD + hh * 96 + 64 + et * 16 + tp * 4);
      v4i16_t k0 = __builtin_amdgcn_ds_read_tr16_b64_v4i16((LAS v4i16_t*)(pk));
      v4i16_t k1 = __builtin_amdgcn_ds_read_tr16_b64_v4i16((LAS v4i16_t*)(pk + 4 * CLD * 2));
      v4i16_t v0 = __builtin_amdgcn_ds_read_tr16_b64_v4i16((LAS v4i16_t*)(pv));
      v4i16_t v1 = __builtin_amdgcn_ds_read_tr16_b64_v4i16((LAS v4i16_t*)(pv + 4 * CLD * 2));
      const bf16x8 kf = {k0[0], k0[1], k0[2], k0[3], k1[0], k1[1], k1[2], k1[3]};
      const bf16x8 vf = {v0[0], v0[1], v0[2], v0[3], v1[0], v1[1], v1[2], v1[3]};
      kv = __builtin_amdgcn_mfma_f32_16x16x32_bf16(kf, vf, kv, 0, 0, 0);
      zz = __builtin_amdgcn_mfma_f32_16x16x32_bf16(kf, ones, zz, 0, 0, 0);
    }
    float* pp = part + ((size_t)bm * H_ + 2 * bn + hh) * 1056;
#pragma unroll
    for (int r = 0; r < 4; ++r) pp[(dt * 16 + 4 * g + r) * 32 + et * 16 + fr] = kv[r];
    if (et == 0 && fr == 0) {
#pragma unroll
      for (int r = 0; r < 4; ++r) pp[1024 + dt * 16 + 4 * g + r] = zz[r];
    }
    return;
  }
  constexpr int CPR = BN / 8;
  constexpr int NCH = Cfg::BM * CPR;
#pragma unroll
  for (int i = tid; i < NCH; i += Cfg::THREADS) {
    const int row = i / CPR, c8 = (i % CPR) * 8;
    u16x8 v = *reinterpret_cast<const u16x8*>(cs + row * CLD + c8);
    const size_t go = (size_t)(m0 + row) * N + n0 + c8;
    if (EPI == EPI_RES) {
      u16x8 rv = *reinterpret_cast<const u16x8*>(res + go);
#pragma unroll
      for (int e = 0; e < 8; e += 2) {
        float t0 = bf2f(v[e]), t1 = bf2f(v[e + 1]);
        const unsigned pk = pk2(bf2f(rv[e]) + t0, bf2f(rv[e + 1]) + t1);
        v[e] = (u16)(pk & 0xffffu); v[e + 1] = (u16)(pk >> 16);
      }
    }
    *reinterpret_cast<u16x8*>(C + go) = v;
  }
}

__global__ __launch_bounds__(256) void o_kernel(const u16* __restrict__ pq, const float* __restrict__ part,
                                                u16* __restrict__ o) {
  __shared__ __attribute__((aligned(16))) float skv[1056];
  const int h = blockIdx.x, rb = blockIdx.y, t = threadIdx.x;
  for (int i = t; i < 1056; i += 256) {
    float s = 0.f;
#pragma unroll
    for (int c = 0; c < 16; ++c) s += part[((size_t)c * H_ + h) * 1056 + i];
    skv[i] = s;
  }
  __syncthreads();
  const int row = rb * 128 + (t >> 1), eh = (t & 1) * 16;
  const u16x8* qp = reinterpret_cast<const u16x8*>(pq + (size_t)row * D_ + h * DH_);
  float q[32];
#pragma unroll
  for (int c = 0; c < 4; ++c) {
    u16x8 a = qp[c];
#pragma unroll
    for (int i = 0; i < 8; ++i) q[c * 8 + i] = bf2f(a[i]);
  }
  float den = 1e-6f;
#pragma unroll
  for (int d = 0; d < 32; ++d) den += q[d] * skv[1024 + d];
  const float inv = 1.f / den;
  float num[16];
#pragma unroll
  for (int e = 0; e < 16; ++e) num[e] = 0.f;
#pragma unroll
  for (int d = 0; d < 32; ++d) {
#pragma unroll
    for (int e4 = 0; e4 < 4; ++e4) {
      const f32x4 kk = *reinterpret_cast<const f32x4*>(&skv[d * 32 + eh + e4 * 4]);
      num[e4 * 4 + 0] += q[d] * kk[0]; num[e4 * 4 + 1] += q[d] * kk[1];
      num[e4 * 4 + 2] += q[d] * kk[2]; num[e4 * 4 + 3] += q[d] * kk[3];
    }
  }
  u16x8 ov[2];
#pragma unroll
  for (int e = 0; e < 16; e += 2) {
    const unsigned pk = pk2(num[e] * inv, num[e + 1] * inv);
    ov[e >> 3][e & 7] = (u16)(pk & 0xffffu); ov[e >> 3][(e & 7) + 1] = (u16)(pk >> 16);
  }
  u16x8* op = reinterpret_cast<u16x8*>(o + (size_t)row * D_ + h * DH_ + eh);
  op[0] = ov[0]; op[1] = ov[1];
}

template <int BN, int WN, int EPI>
static void launch_gemm(const u16* A, const u16* B, u16* C, const u16* bias, const u16* res, float* part, int N,
                        int K, hipStream_t stream) {
  using Cfg = GemmCfg<BN, WN>;
  (void)hipFuncSetAttribute((const void*)gemm_kernel<BN, WN, EPI>, hipFuncAttributeMaxDynamicSharedMemorySize, Cfg::LDS_BYTES);
  const int ntn = N / BN;
  gemm_kernel<BN, WN, EPI><<<(S_ / 128) * ntn * (EPI == EPI_PART ? 4 : 1), Cfg::THREADS, Cfg::LDS_BYTES, stream>>>(A, B, C, bias, res, part, N, K, ntn);
}

extern "C" void kernel_launch(void* const* d_in, const int* in_sizes, int n_in, void* d_out, int out_size,
                              void* d_ws, size_t ws_size, hipStream_t stream) {
  const u16* x0 = (const u16*)d_in[0];
  const u16* wqkv = (const u16*)d_in[1];
  const u16* wo = (const u16*)d_in[2];
  const u16* w1 = (const u16*)d_in[4];
  const u16* b1 = (const u16*)d_in[5];
  const u16* w2 = (const u16*)d_in[6];
  const u16* b2 = (const u16*)d_in[7];
  u16* out = (u16*)d_out;
  char* ws = (char*)d_ws;
  u16* xn = (u16*)(ws);
  u16* qkv = (u16*)(ws + (4u << 20));
  u16* ob = (u16*)(ws + (16u << 20));
  u16* xs = (u16*)(ws + (20u << 20));
  u16* hb = (u16*)(ws + (24u << 20));
  float* part = (float*)(ws + (40u << 20));

  float* pbuf = (float*)(ws + (48u << 20));
  ln_kernel<0, 0, 1><<<S_ / 4, 256, 0, stream>>>(x0, xn, nullptr, nullptr, nullptr);
  for (int layer = 0; layer < 2; ++layer) {
    const u16* xin = layer == 0 ? x0 : xs;
    launch_gemm<192, 4, EPI_QKV>(xn, wqkv, qkv, nullptr, nullptr, part, 3 * D_, D_, stream);
    o_kernel<<<dim3(H_, 16), 256, 0, stream>>>(qkv, part, ob);
    launch_gemm<64, 2, EPI_RES>(ob, wo, xs, nullptr, xin, nullptr, D_, D_, stream);
    ln_kernel<1, 0, 1><<<S_ / 4, 256, 0, stream>>>(xs, xn, nullptr, nullptr, nullptr);
    launch_gemm<256, 4, EPI_GELU>(xn, w1, hb, b1, nullptr, nullptr, F_, D_, stream);
    launch_gemm<256, 4, EPI_PART>(hb, w2, nullptr, nullptr, nullptr, pbuf, D_, F_, stream);
    if (layer == 0) ln_kernel<0, 1, 1><<<S_ / 4, 256, 0, stream>>>(xs, xn, pbuf, b2, xs);
    else ln_kernel<0, 1, 0><<<S_ / 4, 256, 0, stream>>>(xs, xn, pbuf, b2, out);
  }
}
```
